```python
import torch

def _softmax(x, axis=-1):
    return torch.softmax(x, dim=axis)
def _gelu(x):
    return torch.nn.functional.gelu(x, approximate="tanh")
def _silu(x):
    return torch.nn.functional.silu(x)
def _sigmoid(x):
    return torch.sigmoid(x)
def _layernorm(x, axis=-1, eps=1e-5):
    m = x.mean(dim=axis, keepdim=True)
    v = x.var(dim=axis, keepdim=True, unbiased=False)
    return (x - m) / torch.sqrt(v + eps)
def _scatter_add(tgt, idx, src, axis=0):
    return tgt.clone().index_add_(axis, idx, src)
def _segment_sum(src, seg, n):
    out = torch.zeros((n,) + src.shape[1:], dtype=src.dtype)
    return out.index_add_(0, seg, src)
def _pad(x, pw):
    flat = tuple(v for pair in reversed(pw) for v in pair)
    return torch.nn.functional.pad(x, flat)

kernel_name = 'gated_mix__compressed_kv_cla__glu_gated_reglu__decode'

def setup_inputs():

    _w = (lambda a: (lambda t: t.to(torch.bfloat16) if t.is_floating_point() else t)(torch.as_tensor(a)))
    def _A_setup():
        B, H, Sq, Sk, D, M = 4, 32, 1, 4096, 128, 512
        Dc = 4 * D
        return {
            "x": _w((lambda *s: torch.randn(*s, dtype=torch.bfloat16) * 0.05)(B, Sq, M)),
            "c_kv": _w((lambda *s: torch.randn(*s, dtype=torch.bfloat16) * 0.4)(B, Sk, Dc)),
            "w_q": _w((lambda *s: torch.randn(*s, dtype=torch.bfloat16) * 6.4)(M, H, D) * (M ** -0.5)),
            "w_uk": _w((lambda *s: torch.randn(*s, dtype=torch.bfloat16) * 6.4)(Dc, H, D) * (Dc ** -0.5)),
            "w_uv": _w((lambda *s: torch.randn(*s, dtype=torch.bfloat16) * 0.4)(Dc, H, D) * (Dc ** -0.5)),
            "w_o": _w((lambda *s: torch.randn(*s, dtype=torch.bfloat16) * 0.4)(H, D, M) * ((H * D) ** -0.5)),
        }
    _A_in = _A_setup()

    def _B_setup():
        B, S, D, F = 4, 1, 512, 1792
        return {
            "x": _w((lambda *s: torch.randn(*s, dtype=torch.bfloat16) * 0.05)(B, S, D)),
            "W_gate": _w((lambda *s: torch.randn(*s, dtype=torch.bfloat16) * 0.8)(D, F) * (2.0 / D) ** 0.5),
            "W_up": _w((lambda *s: torch.randn(*s, dtype=torch.bfloat16) * 0.8)(D, F) * (2.0 / D) ** 0.5),
            "W_down": _w((lambda *s: torch.randn(*s, dtype=torch.bfloat16) * 0.8)(F, D) * (1.0 / F) ** 0.5),
        }
    _B_in = _B_setup()

    return {
        "A_x": _A_in["x"],
        "A_c_kv": _A_in["c_kv"],
        "A_w_q": _A_in["w_q"],
        "A_w_uk": _A_in["w_uk"],
        "A_w_uv": _A_in["w_uv"],
        "A_w_o": _A_in["w_o"],
        "B_x": _B_in["x"],
        "B_W_gate": _B_in["W_gate"],
        "B_W_up": _B_in["W_up"],
        "B_W_down": _B_in["W_down"],
        "gate_w": _w((lambda *s: torch.randn(*s, dtype=torch.bfloat16) * 0.05)(512) * 16.0),
    }

def reference(A_x, A_c_kv, A_w_q, A_w_uk, A_w_uv, A_w_o, B_x, B_W_gate, B_W_up, B_W_down, gate_w):
    def _A_ref(x, c_kv, w_q, w_uk, w_uv, w_o):
        B, Sq, M = x.shape[0], x.shape[1], x.shape[2]
        Sk = c_kv.shape[1]
        H, D = w_q.shape[1], w_q.shape[2]
        q = torch.einsum("bqm,mhd->bhqd", x, w_q)
        k = torch.einsum("bkc,chd->bhkd", c_kv, w_uk)
        v = torch.einsum("bkc,chd->bhkd", c_kv, w_uv)
        scores = torch.einsum("bhqd,bhkd->bhqk", q, k) * (D ** -0.5)
        off = Sk - Sq
        qi = torch.arange(Sq)[:, None]
        kj = torch.arange(Sk)[None, :]
        cmask = kj <= (qi + off)
        scores = torch.where(cmask[None, None], scores, -1e9)
        p = _softmax(scores, axis=-1)
        ctx = torch.einsum("bhqk,bhkd->bhqd", p, v)
        return torch.einsum("bhqd,hdm->bqm", ctx, w_o)

    def _B_ref(x, W_gate, W_up, W_down):
        gate = torch.einsum("bsd,df->bsf", x, W_gate)
        up = torch.einsum("bsd,df->bsf", x, W_up)
        gate = gate * (lambda x, dt: x.to(dt))(gate > 0, gate.dtype)
        h = gate * up
        return torch.einsum("bsf,fd->bsd", h, W_down)

    x = A_x
    g = _sigmoid((x * gate_w).sum(-1, keepdims=True))
    a = _A_ref(x, A_c_kv, A_w_q, A_w_uk, A_w_uv, A_w_o)
    b = _B_ref(x, B_W_gate, B_W_up, B_W_down)
    return g * a + (1 - g) * b
```

```cpp
#include <hip/hip_runtime.h>
#include <hip/hip_bf16.h>
#include <stdint.h>

namespace {
constexpr int B = 4, H = 32, SK = 4096, D = 128, M = 512, DC = 512, F = 1792;
constexpr int NCH = 64, CHK = 64;
typedef unsigned short u16;
__device__ __forceinline__ float bf2f(u16 v) { return __uint_as_float((unsigned)v << 16); }
__device__ __forceinline__ float rbf(float f) { return __bfloat162float(__float2bfloat16(f)); }

__global__ void k_q(const u16* x, const u16* wq, float* q) {
  __shared__ float red[4][4][64];
  int col = blockIdx.x * 64 + (threadIdx.x & 63), mg = threadIdx.x >> 6;
  float a[4] = {0, 0, 0, 0};
  for (int m = mg * 128; m < mg * 128 + 128; ++m) {
    float w = bf2f(wq[(size_t)m * 4096 + col]);
    for (int b = 0; b < 4; ++b) a[b] += bf2f(x[b * 512 + m]) * w;
  }
  for (int b = 0; b < 4; ++b) red[mg][b][threadIdx.x & 63] = a[b];
  __syncthreads();
  int b = mg, c = threadIdx.x & 63;
  q[b * 4096 + blockIdx.x * 64 + c] = rbf(red[0][b][c] + red[1][b][c] + red[2][b][c] + red[3][b][c]);
}
__global__ void k_qabs(const float* q, const u16* wuk, float* qa) {
  int wid = blockIdx.x * 4 + (threadIdx.x >> 6), l = threadIdx.x & 63;
  int c = wid >> 5, h = wid & 31;
  unsigned w2 = *(const unsigned*)(wuk + ((size_t)c * 32 + h) * 128 + 2 * l);
  float w0 = bf2f(w2 & 0xffff), w1 = bf2f(w2 >> 16);
  for (int b = 0; b < 4; ++b) {
    float s = q[b * 4096 + h * 128 + 2 * l] * w0 + q[b * 4096 + h * 128 + 2 * l + 1] * w1;
    for (int o = 32; o; o >>= 1) s += __shfl_xor(s, o);
    if (l == 0) qa[(b * 32 + h) * 512 + c] = s * 0.08838834764831845f;
  }
}
__global__ void k_attn(const float* qa, const u16* ckv, float* po, float* pm, float* pl) {
  __shared__ float s[32][CHK + 1];
  __shared__ u16 ct[CHK][512 + 8];
  int b = blockIdx.x / NCH, j = blockIdx.x % NCH, t = threadIdx.x;
  const u16* src = ckv + ((size_t)b * SK + j * CHK) * 512;
  for (int i = t; i < CHK * 64; i += 256) { int r = i >> 6, c8 = i & 63; *(uint4*)&ct[r][c8 * 8] = *(const uint4*)(src + r * 512 + c8 * 8); }
  __syncthreads();
  for (int i = t; i < 32 * CHK; i += 256) {
    int h = i / CHK, k = i % CHK; float a = 0; const float* qq = qa + (b * 32 + h) * 512;
    for (int c = 0; c < 512; ++c) a += qq[c] * bf2f(ct[k][c]);
    s[h][k] = a;
  }
  __syncthreads();
  if (t < 32) {
    float m = -1e30f; for (int k = 0; k < CHK; ++k) m = fmaxf(m, s[t][k]);
    float l = 0; for (int k = 0; k < CHK; ++k) { float p = __expf(s[t][k] - m); s[t][k] = p; l += p; }
    pm[(b * NCH + j) * 32 + t] = m; pl[(b * NCH + j) * 32 + t] = l;
  }
  __syncthreads();
  for (int i = t; i < 32 * 512; i += 256) {
    int h = i >> 9, c = i & 511; float a = 0;
    for (int k = 0; k < CHK; ++k) a += s[h][k] * bf2f(ct[k][c]);
    po[((size_t)(b * NCH + j) * 32 + h) * 512 + c] = a;
  }
}
__global__ void k_comb(const float* po, const float* pm, const float* pl, const u16* wuv, float* ctx) {
  __shared__ float o[512]; __shared__ float w[NCH]; __shared__ float red[4][128];
  int b = blockIdx.x >> 5, h = blockIdx.x & 31, t = threadIdx.x;
  if (t == 0) {
    float m = -1e30f; for (int j = 0; j < NCH; ++j) m = fmaxf(m, pm[(b * NCH + j) * 32 + h]);
    float L = 0; for (int j = 0; j < NCH; ++j) { float e = __expf(pm[(b * NCH + j) * 32 + h] - m); w[j] = e; L += e * pl[(b * NCH + j) * 32 + h]; }
    for (int j = 0; j < NCH; ++j) w[j] /= L;
  }
  __syncthreads();
  float a = 0; for (int j = 0; j < NCH; ++j) a += w[j] * po[((size_t)(b * NCH + j) * 32 + h) * 512 + t];
  o[t] = a; __syncthreads();
  int d = t & 127, cg = t >> 7; float s = 0;
  for (int c = cg * 128; c < cg * 128 + 128; ++c) s += o[c] * bf2f(wuv[((size_t)c * 32 + h) * 128 + d]);
  red[cg][d] = s; __syncthreads();
  if (t < 128) ctx[b * 4096 + h * 128 + t] = rbf(red[0][t] + red[1][t] + red[2][t] + red[3][t]);
}
__global__ void k_ffn1(const u16* x, const u16* wg, const u16* wu, float* hh) {
  __shared__ float red[2][4][4][64];
  int col = blockIdx.x * 64 + (threadIdx.x & 63), mg = threadIdx.x >> 6;
  float a[4] = {0, 0, 0, 0}, u[4] = {0, 0, 0, 0};
  for (int m = mg * 128; m < mg * 128 + 128; ++m) {
    float w = bf2f(wg[(size_t)m * F + col]), w2 = bf2f(wu[(size_t)m * F + col]);
    for (int b = 0; b < 4; ++b) { float xv = bf2f(x[b * 512 + m]); a[b] += xv * w; u[b] += xv * w2; }
  }
  for (int b = 0; b < 4; ++b) { red[0][mg][b][threadIdx.x & 63] = a[b]; red[1][mg][b][threadIdx.x & 63] = u[b]; }
  __syncthreads();
  int b = mg, c = threadIdx.x & 63;
  float g = rbf(red[0][0][b][c] + red[0][1][b][c] + red[0][2][b][c] + red[0][3][b][c]);
  float uu = rbf(red[1][0][b][c] + red[1][1][b][c] + red[1][2][b][c] + red[1][3][b][c]);
  g = g > 0 ? g : 0.f;
  hh[b * F + blockIdx.x * 64 + c] = rbf(g * uu);
}
__global__ void k_out(const u16* x, const u16* gw, const float* ctx, const u16* wo, const float* hh, const u16* wd, __hip_bfloat16* out) {
  __shared__ float red[2][16][4][64]; __shared__ float gs[4];
  int c = threadIdx.x & 63, kg = threadIdx.x >> 6, col = blockIdx.x * 64 + c;
  float a[4] = {0, 0, 0, 0}, bb[4] = {0, 0, 0, 0};
  for (int k = kg * 256; k < kg * 256 + 256; ++k) { float w = bf2f(wo[(size_t)k * 512 + col]); for (int b = 0; b < 4; ++b) a[b] += ctx[b * 4096 + k] * w; }
  for (int k = kg * 112; k < kg * 112 + 112; ++k) { float w = bf2f(wd[(size_t)k * 512 + col]); for (int b = 0; b < 4; ++b) bb[b] += hh[b * F + k] * w; }
  for (int b = 0; b < 4; ++b) { red[0][kg][b][c] = a[b]; red[1][kg][b][c] = bb[b]; }
  if (kg < 4) { float s = 0; for (int m = c; m < 512; m += 64) s += rbf(bf2f(x[kg * 512 + m]) * bf2f(gw[m])); for (int o = 32; o; o >>= 1) s += __shfl_xor(s, o); if (c == 0) gs[kg] = rbf(1.f / (1.f + __expf(-rbf(s)))); }
  __syncthreads();
  if (kg < 4) {
    int b = kg; float sa = 0, sb = 0; for (int i = 0; i < 16; ++i) { sa += red[0][i][b][c]; sb += red[1][i][b][c]; }
    float g = gs[b]; out[b * 512 + col] = __float2bfloat16(rbf(g * rbf(sa)) + rbf(rbf(1.f - g) * rbf(sb)));
  }
}
}

extern "C" void kernel_launch(void* const* d_in, const int* in_sizes, int n_in, void* d_out, int out_size, void* d_ws, size_t ws_size, hipStream_t stream) {
  const u16 *x = (const u16*)d_in[0], *ckv = (const u16*)d_in[1], *wq = (const u16*)d_in[2], *wuk = (const u16*)d_in[3], *wuv = (const u16*)d_in[4], *wo = (const u16*)d_in[5];
  const u16 *wg = (const u16*)d_in[7], *wu = (const u16*)d_in[8], *wd = (const u16*)d_in[9], *gw = (const u16*)d_in[10];
  float* ws = (float*)d_ws;
  float *q = ws, *qa = q + 4 * 4096, *ctx = qa + 4 * 32 * 512, *hh = ctx + 4 * 4096, *pm = hh + 4 * 2048, *pl = pm + 4 * NCH * 32, *po = pl + 4 * NCH * 32;
  k_q<<<64, 256, 0, stream>>>(x, wq, q);
  k_qabs<<<512 * 32 / 4, 256, 0, stream>>>(q, wuk, qa);
  k_ffn1<<<F / 64, 256, 0, stream>>>(x, wg, wu, hh);
  k_attn<<<4 * NCH, 256, 0, stream>>>(qa, ckv, po, pm, pl);
  k_comb<<<128, 512, 0, stream>>>(po, pm, pl, wuv, ctx);
  k_out<<<8, 1024, 0, stream>>>(x, gw, ctx, wo, hh, wd, (__hip_bfloat16*)d_out);
}
```

```cpp
#include <hip/hip_runtime.h>
#include <hip/hip_bf16.h>
#include <stdint.h>

namespace {
typedef unsigned short u16;
typedef unsigned v4u __attribute__((ext_vector_type(4)));
typedef unsigned v2u __attribute__((ext_vector_type(2)));
typedef float f32x2 __attribute__((ext_vector_type(2)));
typedef float f32x4 __attribute__((ext_vector_type(4)));
typedef float f32x16 __attribute__((ext_vector_type(16)));
typedef short bf16x8 __attribute__((ext_vector_type(8)));
typedef short bf16x4 __attribute__((ext_vector_type(4)));
#define LAS __attribute__((address_space(3)))

constexpr int NWG = 256, NT = 512;
constexpr int FF = 1792;
constexpr unsigned WS_EPOCH = 0;
constexpr unsigned WS_QA = 4096;
constexpr unsigned WS_OP = WS_QA + 4 * 32 * 2048;
constexpr unsigned WS_ST = WS_OP + 4 * 32 * 2 * 32 * 1024;
constexpr unsigned WS_F = WS_ST + 4 * 32 * 32 * 16;
constexpr unsigned WS_FIN = WS_F + 4 * 56 * 4096;
constexpr unsigned WS_BYTES = WS_FIN + 4 * 64 * 4096;
constexpr int AUX_SC1 = 16;
constexpr unsigned SPIN_CAP = 1u << 17;

constexpr int ROFF = 131072;
constexpr int R_P = ROFF;
constexpr int R_STG = ROFF + 4096;
constexpr int X_S1 = ROFF + 20480;
constexpr int X_CTX = X_S1;
constexpr int X_RED = X_S1 + 4096;
constexpr int X_F1 = ROFF + 28672;
constexpr int X_FR = X_F1;
constexpr int X_MX = ROFF + 30720;
constexpr int X_G = ROFF + 31744;
constexpr int X_SYNC = X_G + 16;
constexpr int X_FIN = X_G + 64;
constexpr int X_WV = ROFF + 16384;
static_assert(X_FIN + 128 <= 163840, "lds");

__device__ __forceinline__ float bf2f(unsigned v) { return __uint_as_float(v << 16); }
__device__ __forceinline__ unsigned bfbits(float f) { return __builtin_bit_cast(unsigned short, __float2bfloat16(f)); }
__device__ __forceinline__ float rbf(float f) { return bf2f(bfbits(f)); }
__device__ __forceinline__ unsigned pk2(float a, float b) { return bfbits(a) | (bfbits(b) << 16); }
__device__ __forceinline__ float lo16(unsigned v) { return __uint_as_float(v << 16); }
__device__ __forceinline__ float hi16(unsigned v) { return __uint_as_float(v & 0xffff0000u); }
__device__ __forceinline__ v4u ldnt(const u16* p) { return __builtin_nontemporal_load((const v4u*)p); }
#define LDSH(p) ldnt(p)
__device__ __forceinline__ int swz16(int r) { return ((r & 3) << 2) | ((((~((r >> 3) ^ (r >> 2))) & 1) << 1) | ((r >> 3) & 1)); }
__device__ __forceinline__ f32x4 mfma16(bf16x8 a, bf16x8 b, f32x4 c) { return __builtin_amdgcn_mfma_f32_16x16x32_bf16(a, b, c, 0, 0, 0); }
__device__ __forceinline__ f32x16 mfma32(bf16x8 a, bf16x8 b, f32x16 c) { return __builtin_amdgcn_mfma_f32_32x32x16_bf16(a, b, c, 0, 0, 0); }
template <int CTRL> __device__ __forceinline__ float dpp(float v) {
  return __uint_as_float(__builtin_amdgcn_update_dpp(0, __float_as_uint(v), CTRL, 0xf, 0xf, true));
}
__device__ __forceinline__ float x32(float a) {
  auto r = __builtin_amdgcn_permlane32_swap(__float_as_uint(a), __float_as_uint(a), false, false);
  return __uint_as_float((threadIdx.x & 32) ? r[0] : r[1]);
}
__device__ __forceinline__ float x16(float a) {
  auto r = __builtin_amdgcn_permlane16_swap(__float_as_uint(a), __float_as_uint(a), false, false);
  return __uint_as_float((threadIdx.x & 16) ? r[0] : r[1]);
}
__device__ __forceinline__ float row_sum(float v) {
  v += dpp<0xB1>(v); v += dpp<0x4E>(v); v += dpp<0x141>(v); v += dpp<0x140>(v); return v;
}
__device__ __forceinline__ float row_max(float v) {
  v = fmaxf(v, dpp<0xB1>(v)); v = fmaxf(v, dpp<0x4E>(v)); v = fmaxf(v, dpp<0x141>(v)); v = fmaxf(v, dpp<0x140>(v)); return v;
}
template <int S> __device__ __forceinline__ bf16x8 colfrag(const v4u* w) {
  constexpr unsigned sel = (S & 1) ? 0x07060302u : 0x05040100u;
  v4u r;
#pragma unroll
  for (int i = 0; i < 4; ++i) r[i] = __builtin_amdgcn_perm(w[2 * i + 1][S >> 1], w[2 * i][S >> 1], sel);
  return __builtin_bit_cast(bf16x8, r);
}
__device__ __forceinline__ void gemv_block(bf16x8 a, const v4u* w, f32x4 (&acc)[8]) {
  acc[0] = mfma16(a, colfrag<0>(w), acc[0]); acc[1] = mfma16(a, colfrag<1>(w), acc[1]);
  acc[2] = mfma16(a, colfrag<2>(w), acc[2]); acc[3] = mfma16(a, colfrag<3>(w), acc[3]);
  acc[4] = mfma16(a, colfrag<4>(w), acc[4]); acc[5] = mfma16(a, colfrag<5>(w), acc[5]);
  acc[6] = mfma16(a, colfrag<6>(w), acc[6]); acc[7] = mfma16(a, colfrag<7>(w), acc[7]);
}
__device__ __forceinline__ void to_frags(v4u* w) {
  v4u r[8];
  r[0] = __builtin_bit_cast(v4u, colfrag<0>(w)); r[1] = __builtin_bit_cast(v4u, colfrag<1>(w));
  r[2] = __builtin_bit_cast(v4u, colfrag<2>(w)); r[3] = __builtin_bit_cast(v4u, colfrag<3>(w));
  r[4] = __builtin_bit_cast(v4u, colfrag<4>(w)); r[5] = __builtin_bit_cast(v4u, colfrag<5>(w));
  r[6] = __builtin_bit_cast(v4u, colfrag<6>(w)); r[7] = __builtin_bit_cast(v4u, colfrag<7>(w));
#pragma unroll
  for (int i = 0; i < 8; ++i) w[i] = r[i];
}
__device__ __forceinline__ void gemv_frags(bf16x8 a, const v4u* f, f32x4 (&acc)[8]) {
#pragma unroll
  for (int i = 0; i < 8; ++i) acc[i] = mfma16(a, __builtin_bit_cast(bf16x8, f[i]), acc[i]);
}
#define SB0() __builtin_amdgcn_sched_barrier(0)
#define TAGS_OK(u) (((u)[0] == T) & ((u)[2] == T))
#define LDU(off) __builtin_amdgcn_raw_buffer_load_b128(rs, (off), 0, AUX_SC1)
#define STU(v, off) __builtin_amdgcn_raw_buffer_store_b128((v), rs, (off), 0, AUX_SC1)

__global__ void __launch_bounds__(NT, 2)
fused_kernel(const u16* __restrict__ x, const u16* __restrict__ ckv, const u16* __restrict__ wq, const u16* __restrict__ wuk,
             const u16* __restrict__ wuv, const u16* __restrict__ wo, const u16* __restrict__ wg, const u16* __restrict__ wu,
             const u16* __restrict__ wd, const u16* __restrict__ gw, u16* __restrict__ out, unsigned char* ws) {
  __shared__ __attribute__((aligned(16))) unsigned char lds[163840];
  const int tid = threadIdx.x, lane = tid & 63, wave = __builtin_amdgcn_readfirstlane(tid >> 6);
  const int bid = blockIdx.x, xcd = bid & 7, idx = bid >> 3;
  const int hA = xcd * 4 + (idx >> 3), cs = idx & 7;
  const int pid = xcd * 16 + (idx >> 1), b3 = pid >> 5, j3 = pid & 31, hf3 = idx & 1;
  const int bB = (idx >> 1) & 3, chB = idx & 1, ii = hA * 2 + chB;
  const int r15 = lane & 15, g4 = lane >> 4, hh = lane >> 5;
  const __amdgpu_buffer_rsrc_t rs = __builtin_amdgcn_make_buffer_rsrc(ws, 0, WS_BYTES, 0x00020000);
  const f32x4 Z4 = {0.f, 0.f, 0.f, 0.f};
  const v4u ZU = {0u, 0u, 0u, 0u};
  const bf16x8 ZB = {0, 0, 0, 0, 0, 0, 0, 0};
  const unsigned epoch = __builtin_amdgcn_raw_buffer_load_b32(rs, WS_EPOCH, 0, AUX_SC1);
  if (tid == 0) *(volatile LAS unsigned*)(lds + X_SYNC) = 0u;
  __syncthreads();
  v4u big[32];
  unsigned T;
  if (wave >= 4) {
    const u16* p = ckv + ((size_t)b3 * 4096 + j3 * 128 + 32 * (wave - 4)) * 512 + lane * 8;
#pragma unroll
    for (int i = 0; i < 32; ++i) big[i] = ldnt(p + i * 512);
    SB0();
    T = __builtin_amdgcn_readfirstlane(epoch) + 1u;
#pragma unroll
    for (int i = 0; i < 32; ++i)
      *(v4u*)(lds + (32 * (wave - 4) + i) * 1024 + ((lane ^ swz16(i & 15)) << 4)) = big[i];
  } else {
    v4u xa[4] = {ZU, ZU, ZU, ZU}, aux[4];
    const int m0 = 128 * wave + 8 * g4;
    if (r15 < 4) {
#pragma unroll
      for (int k = 0; k < 4; ++k) xa[k] = *(const v4u*)(x + r15 * 512 + m0 + 32 * k);
    }
    {
      const u16* p = wq + (size_t)m0 * 4096 + hA * 128 + r15 * 8;
#pragma unroll
      for (int i = 0; i < 32; ++i) big[i] = LDSH(p + (size_t)((i >> 3) * 32 + (i & 7)) * 4096);
      const u16* q = wuk + ((size_t)(cs * 64 + 16 * wave + r15) * 32 + hA) * 128 + g4 * 8;
#pragma unroll
      for (int k = 0; k < 4; ++k) aux[k] = ldnt(q + k * 32);
    }
    SB0();
    T = __builtin_amdgcn_readfirstlane(epoch) + 1u;
    {
      f32x4 acc[8] = {Z4, Z4, Z4, Z4, Z4, Z4, Z4, Z4};
#pragma unroll
      for (int k = 0; k < 4; ++k) gemv_block(__builtin_bit_cast(bf16x8, xa[k]), big + 8 * k, acc);
      if (lane < 16) {
#pragma unroll
        for (int b = 0; b < 4; ++b) {
          float* d = (float*)(lds + X_S1) + (wave * 4 + b) * 128 + lane * 8;
          *(f32x4*)d = f32x4{acc[0][b], acc[1][b], acc[2][b], acc[3][b]};
          *(f32x4*)(d + 4) = f32x4{acc[4][b], acc[5][b], acc[6][b], acc[7][b]};
        }
      }
    }
    __builtin_amdgcn_fence(__ATOMIC_RELEASE, "workgroup");
    if (lane == 0) __hip_atomic_fetch_add((LAS unsigned*)(lds + X_SYNC), 1u, __ATOMIC_RELAXED, __HIP_MEMORY_SCOPE_WORKGROUP);
    while (__hip_atomic_load((LAS unsigned*)(lds + X_SYNC), __ATOMIC_RELAXED, __HIP_MEMORY_SCOPE_WORKGROUP) < 4u) __builtin_amdgcn_s_sleep(1);
    __builtin_amdgcn_fence(__ATOMIC_ACQUIRE, "workgroup");
    {
      f32x4 acc = Z4;
#pragma unroll
      for (int k = 0; k < 4; ++k) {
        v4u bq = ZU;
        if (r15 < 4) {
          f32x4 s0 = Z4, s1 = Z4;
#pragma unroll
          for (int w = 0; w < 4; ++w) {
            const float* p = (const float*)(lds + X_S1) + (w * 4 + r15) * 128 + 32 * k + 8 * g4;
            s0 += *(const f32x4*)p; s1 += *(const f32x4*)(p + 4);
          }
          bq = v4u{pk2(s0[0], s0[1]), pk2(s0[2], s0[3]), pk2(s1[0], s1[1]), pk2(s1[2], s1[3])};
        }
        acc = mfma16(__builtin_bit_cast(bf16x8, aux[k]), __builtin_bit_cast(bf16x8, bq), acc);
      }
      const float sc = 0.08838834764831845f * 1.4426950408889634f;
      if (r15 < 4) STU((v4u{T, pk2(acc[0] * sc, acc[1] * sc), T, pk2(acc[2] * sc, acc[3] * sc)}), WS_QA + (unsigned)((r15 * 32 + hA) * 2048 + (cs * 64 + 16 * wave + 4 * g4) * 4));
    }
    if (wave == 2) {
      if (bid < 224) {
        const int fs = bid >> 3, mr = bid & 7, f0 = 64 * mr + 8 * g4;
        v4u xa0 = ZU, xa1 = ZU;
        if (r15 < 4) { xa0 = *(const v4u*)(x + r15 * 512 + f0); xa1 = *(const v4u*)(x + r15 * 512 + f0 + 32); }
        const u16* q = (r15 < 8 ? wg : wu) + (size_t)f0 * FF + 64 * fs + 8 * (r15 & 7);
#pragma unroll
        for (int i = 0; i < 8; ++i) { big[i] = ldnt(q + (size_t)i * FF); big[8 + i] = ldnt(q + (size_t)(32 + i) * FF); }
        f32x4 acc[8] = {Z4, Z4, Z4, Z4, Z4, Z4, Z4, Z4};
        gemv_block(__builtin_bit_cast(bf16x8, xa0), big, acc);
        gemv_block(__builtin_bit_cast(bf16x8, xa1), big + 8, acc);
        unsigned char* priv = lds + X_F1;
        if (lane < 16) {
#pragma unroll
          for (int b = 0; b < 4; ++b) {
            float* d = (float*)priv + b * 128 + lane * 8;
            *(f32x4*)d = f32x4{acc[0][b], acc[1][b], acc[2][b], acc[3][b]};
            *(f32x4*)(d + 4) = f32x4{acc[4][b], acc[5][b], acc[6][b], acc[7][b]};
          }
        }
#pragma unroll
        for (int b = 0; b < 4; ++b) {
          const f32x2 u = *(const f32x2*)(priv + (b * 64 + lane) * 8);
          const int fp = lane & 31;
          STU((v4u{T, __float_as_uint(u[0]), T, __float_as_uint(u[1])}), WS_F + (unsigned)(((b * 56 + 2 * fs + (fp >> 4)) * 256 + mr * 32 + hh * 16 + (fp & 15)) * 16));
        }
      }
    } else if (wave == 3) {
      const v4u xr = *(const v4u*)(x + bB * 512 + lane * 8), gr = *(const v4u*)(gw + lane * 8);
      float s = 0.f;
#pragma unroll
      for (int j = 0; j < 4; ++j) s += rbf(lo16(xr[j]) * lo16(gr[j])) + rbf(hi16(xr[j]) * hi16(gr[j]));
      s = row_sum(s); s += x16(s); s += x32(s);
      if (lane == 0) {
        const float g = rbf(1.f / (1.f + __expf(-rbf(s))));
        *(f32x2*)(lds + X_G) = f32x2{g, rbf(1.f - g)};
      }
    }
  }
  {
    v4u qg[4];
    for (unsigned spins = 0;; ++spins) {
      bool ok = true;
#pragma unroll
      for (int k = 0; k < 4; ++k) qg[k] = LDU(WS_QA + (unsigned)((b3 * 32 + 16 * hf3 + 2 * wave + (k >> 1)) * 2048 + (k & 1) * 1024 + lane * 16));
#pragma unroll
      for (int k = 0; k < 4; ++k) ok &= TAGS_OK(qg[k]);
      if (__all(ok) || spins > SPIN_CAP) break;
      __builtin_amdgcn_s_sleep(4);
    }
#pragma unroll
    for (int k = 0; k < 4; ++k) {
      const int h = 2 * wave + (k >> 1), pi = (k & 1) * 64 + lane, ch = pi >> 1;
      *(v2u*)(lds + ROFF + h * 1024 + ((ch ^ swz16(h)) << 4) + (pi & 1) * 8) = v2u{qg[k][1], qg[k][3]};
    }
  }
  __syncthreads();
  const f32x2 gate = *(const f32x2*)(lds + X_G);
  f32x4 sacc = Z4;
  const bool doF2 = wave < 4 && ii < 56;
  {
    const int sw = swz16(r15);
    const u16* pv = wuv + ((size_t)(chB * 256 + 32 * wave + 8 * g4) * 32 + hA) * 128 + r15 * 8;
    const u16* po = wo + (size_t)(hA * 128 + 64 * (wave >> 2) + 8 * g4) * 512 + 128 * (wave & 3) + r15 * 8;
    const u16* pd = wd + (size_t)((doF2 ? ii : 0) * 32 + 8 * g4) * 512 + 128 * (wave & 3) + r15 * 8;
#pragma unroll
    for (int ks = 0; ks < 16; ++ks) {
#pragma unroll
      for (int e = 0; e < 2; ++e) {
        const int i = 2 * ks + e;
        if (i < 8) big[i] = LDSH(pv + (size_t)i * 4096);
        else if (i < 16) big[i] = LDSH(po + (i - 8) * 512);
        else if (i < 24) big[i] = LDSH(po + (32 + i - 16) * 512);
        else if (doF2) big[i] = LDSH(pd + (i - 24) * 512);
      }
      const int o = r15 * 1024 + (((4 * (ks & 3) + g4) ^ sw) << 4) + (ks >> 2) * 256;
      sacc = mfma16(*(const bf16x8*)(lds + wave * 16384 + o), *(const bf16x8*)(lds + ROFF + o), sacc);
      SB0();
    }
  }
  {
    float m0 = fmaxf(fmaxf(sacc[0], sacc[1]), fmaxf(sacc[2], sacc[3]));
    m0 = fmaxf(m0, x16(m0)); m0 = fmaxf(m0, x32(m0));
    if (lane < 16) *(float*)(lds + X_MX + (wave * 16 + lane) * 4) = m0;
  }
  __syncthreads();
  float mxh = -3.0e38f;
  {
#pragma unroll
    for (int w = 0; w < 8; ++w) mxh = fmaxf(mxh, *(const float*)(lds + X_MX + (w * 16 + r15) * 4));
    const float p0 = exp2f(sacc[0] - mxh), p1 = exp2f(sacc[1] - mxh), p2 = exp2f(sacc[2] - mxh), p3 = exp2f(sacc[3] - mxh);
    float ls = (p0 + p1) + (p2 + p3);
    *(v2u*)(lds + R_P + r15 * 256 + (((2 * wave + (g4 >> 1)) ^ swz16(r15)) << 4) + (g4 & 1) * 8) = v2u{pk2(p0, p1), pk2(p2, p3)};
    ls += x16(ls); ls += x32(ls);
    if (lane < 16) *(float*)(lds + X_MX + 512 + (wave * 16 + lane) * 4) = ls;
  }
  __syncthreads();
  {
    if (wave == 0 && lane < 16) {
      float l = 0.f;
#pragma unroll
      for (int w = 0; w < 8; ++w) l += *(const float*)(lds + X_MX + 512 + (w * 16 + lane) * 4);
      STU((v4u{T, __float_as_uint(mxh), T, __float_as_uint(l)}), WS_ST + (unsigned)(((b3 * 32 + 16 * hf3 + lane) * 32 + j3) * 16));
    }
    const int q = r15 >> 2, p = lane & 3;
    bf16x8 pb[4];
#pragma unroll
    for (int ks = 0; ks < 4; ++ks) pb[ks] = *(const bf16x8*)(lds + R_P + r15 * 256 + (((4 * ks + g4) ^ swz16(r15)) << 4));
    unsigned char* stg = lds + R_STG + wave * 2048;
#pragma unroll
    for (int cp = 0; cp < 2; ++cp) {
#pragma unroll
      for (int c2 = 0; c2 < 2; ++c2) {
        const int cb = 2 * cp + c2;
        const int chunk = 8 * wave + 2 * cb + (p >> 1);
        f32x4 o = Z4;
#pragma unroll
        for (int ks = 0; ks < 4; ++ks) {
          const int r0 = 8 * g4 + q, r1 = r0 + 4;
          const bf16x4 a0 = __builtin_amdgcn_ds_read_tr16_b64_v4i16((LAS bf16x4*)(lds + (32 * ks + r0) * 1024 + ((chunk ^ swz16(r0 & 15)) << 4) + 8 * (p & 1)));
          const bf16x4 a1 = __builtin_amdgcn_ds_read_tr16_b64_v4i16((LAS bf16x4*)(lds + (32 * ks + r1) * 1024 + ((chunk ^ swz16(r1 & 15)) << 4) + 8 * (p & 1)));
          o = mfma16(bf16x8{a0[0], a0[1], a0[2], a0[3], a1[0], a1[1], a1[2], a1[3]}, pb[ks], o);
        }
        *(v4u*)(stg + r15 * 128 + (((4 * c2 + g4) ^ (r15 & 7)) << 4)) = v4u{T, pk2(o[0], o[1]), T, pk2(o[2], o[3])};
      }
#pragma unroll
      for (int i = 0; i < 2; ++i) {
        const int h2 = 8 * i + (lane >> 3), u = lane & 7;
        const v4u o = *(const v4u*)(stg + h2 * 128 + ((u ^ (h2 & 7)) << 4));
        STU(o, WS_OP + (unsigned)(((((b3 * 32 + 16 * hf3 + h2) * 2 + (wave >> 2)) * 32 + j3) * 1024) + (wave & 3) * 256 + cp * 128 + u * 16));
      }
    }
  }
  to_frags(big); to_frags(big + 8); to_frags(big + 16);
  if (doF2) to_frags(big + 24);
  SB0();
  unsigned char* wv;
  {
    v4u og[4], st;
    v4u fg = {T, 0u, T, 0u};
    const bool getF = ii < 56 && wave < 4;
    if (getF) fg = LDU(WS_F + (unsigned)(bB * 56 + ii) * 4096u + tid * 16u);
    for (unsigned spins = 0;; ++spins) {
      st = LDU(WS_ST + (unsigned)(((bB * 32 + hA) * 32 + (lane & 31)) * 16));
      if (__all(TAGS_OK(st)) || spins > SPIN_CAP) break;
      __builtin_amdgcn_s_sleep(2);
    }
    for (unsigned spins = 0;; ++spins) {
      bool ok = true;
#pragma unroll
      for (int i = 0; i < 4; ++i) og[i] = LDU(WS_OP + (unsigned)((((bB * 32 + hA) * 2 + chB) * 32 + 8 * i + (lane >> 3)) * 1024 + wave * 128 + (lane & 7) * 16));
      if (getF && !__all(TAGS_OK(fg))) fg = LDU(WS_F + (unsigned)(bB * 56 + ii) * 4096u + tid * 16u);
#pragma unroll
      for (int i = 0; i < 4; ++i) ok &= TAGS_OK(og[i]);
      ok &= TAGS_OK(fg);
      if (__all(ok) || spins > SPIN_CAP) break;
      __builtin_amdgcn_s_sleep(2);
    }
    __syncthreads();
    wv = lds + X_WV + wave * 512;
    const float mj = __uint_as_float(st[1]), lj = __uint_as_float(st[3]);
    float mm = row_max(mj); mm = fmaxf(mm, x16(mm));
    const float e = exp2f(mj - mm);
    float L = row_sum(e * lj); L += x16(L);
    if (lane < 32) *(float*)(wv + lane * 4) = e / L;
    f32x4 ov = Z4;
#pragma unroll
    for (int i = 0; i < 4; ++i) {
      const float w = *(const float*)(wv + (8 * i + (lane >> 3)) * 4);
      ov[0] += lo16(og[i][1]) * w; ov[1] += hi16(og[i][1]) * w; ov[2] += lo16(og[i][3]) * w; ov[3] += hi16(og[i][3]) * w;
    }
#pragma unroll
    for (int c = 0; c < 4; ++c) { ov[c] += dpp<0x128>(ov[c]); ov[c] += x16(ov[c]); ov[c] += x32(ov[c]); }
    if (lane < 8) {
      const v2u hi = {pk2(ov[0], ov[1]), pk2(ov[2], ov[3])};
      const v2u lo = {pk2(ov[0] - lo16(hi[0]), ov[1] - hi16(hi[0])), pk2(ov[2] - lo16(hi[1]), ov[3] - hi16(hi[1]))};
      *(v2u*)(wv + 128 + lane * 8) = hi;
      *(v2u*)(wv + 192 + lane * 8) = lo;
    }
    if (getF) *(f32x2*)(lds + X_FR + tid * 8) = f32x2{__uint_as_float(fg[1]), __uint_as_float(fg[3])};
  }
  {
    bf16x8 a = ZB;
    if (r15 < 2) a = *(const bf16x8*)(wv + 128 + r15 * 64 + g4 * 16);
    f32x4 acc[8] = {Z4, Z4, Z4, Z4, Z4, Z4, Z4, Z4};
    gemv_frags(a, big, acc);
    if (lane < 16) {
      float* d = (float*)(lds + X_CTX) + wave * 128 + lane * 8;
      *(f32x4*)d = f32x4{acc[0][0] + acc[0][1], acc[1][0] + acc[1][1], acc[2][0] + acc[2][1], acc[3][0] + acc[3][1]};
      *(f32x4*)(d + 4) = f32x4{acc[4][0] + acc[4][1], acc[5][0] + acc[5][1], acc[6][0] + acc[6][1], acc[7][0] + acc[7][1]};
    }
  }
  __syncthreads();
  {
    float cx = 0.f;
#pragma unroll
    for (int w = 0; w < 8; ++w) cx += *(const float*)(lds + X_CTX + (w * 128 + 64 * (wave >> 2) + lane) * 4);
    const unsigned chi = bfbits(cx);
    *(u16*)(wv + lane * 2) = (u16)chi;
    *(u16*)(wv + 128 + lane * 2) = (u16)bfbits(cx - bf2f(chi));
    if (doF2 && lane < 32) {
      float ga = 0.f, ua = 0.f;
#pragma unroll
      for (int w = 0; w < 8; ++w) { ga += *(const float*)(lds + X_FR + (w * 64 + lane) * 4); ua += *(const float*)(lds + X_FR + (w * 64 + 32 + lane) * 4); }
      ga = rbf(ga); ua = rbf(ua);
      ga = ga > 0.f ? ga : 0.f;
      *(u16*)(wv + 256 + lane * 2) = (u16)bfbits(ga * ua);
    }
    bf16x8 a0 = ZB, a1 = ZB, a2 = ZB;
    if (r15 < 2) { a0 = *(const bf16x8*)(wv + r15 * 128 + g4 * 16); a1 = *(const bf16x8*)(wv + r15 * 128 + 64 + g4 * 16); }
    f32x4 acc[8] = {Z4, Z4, Z4, Z4, Z4, Z4, Z4, Z4};
    gemv_frags(a0, big + 8, acc);
    gemv_frags(a1, big + 16, acc);
    if (doF2) {
      if (r15 == 2) a2 = *(const bf16x8*)(wv + 256 + g4 * 16);
      gemv_frags(a2, big + 24, acc);
    }
    if (lane < 16) {
      float r[8];
#pragma unroll
      for (int s = 0; s < 8; ++s) r[s] = gate[0] * (acc[s][0] + acc[s][1]) + gate[1] * acc[s][2];
      float* d = (float*)(lds + X_RED) + (wave >> 2) * 512 + 128 * (wave & 3) + lane * 8;
      *(f32x4*)d = f32x4{r[0], r[1], r[2], r[3]};
      *(f32x4*)(d + 4) = f32x4{r[4], r[5], r[6], r[7]};
    }
  }
  __syncthreads();
  if (tid < 256) {
    const f32x2 u0 = *(const f32x2*)(lds + X_RED + tid * 8), u1 = *(const f32x2*)(lds + X_RED + 2048 + tid * 8);
    STU((v4u{T, __float_as_uint(u0[0] + u1[0]), T, __float_as_uint(u0[1] + u1[1])}), WS_FIN + (unsigned)(((bB * 64 + (tid >> 2)) * 64 + ii) * 4 + (tid & 3)) * 16u);
    v4u fv;
    for (unsigned spins = 0;; ++spins) {
      fv = LDU(WS_FIN + (unsigned)(bB * 64 + ii) * 4096u + tid * 16u);
      if (__all(TAGS_OK(fv)) || spins > SPIN_CAP) break;
      __builtin_amdgcn_s_sleep(1);
    }
    float v0 = __uint_as_float(fv[1]), v1 = __uint_as_float(fv[3]);
    v0 += dpp<0x124>(v0); v1 += dpp<0x124>(v1);
    v0 += dpp<0x128>(v0); v1 += dpp<0x128>(v1);
    v0 += x16(v0); v1 += x16(v1);
    v0 += x32(v0); v1 += x32(v1);
    if (lane < 4) *(f32x2*)(lds + X_FIN + (wave * 8 + lane * 2) * 4) = f32x2{v0, v1};
  }
  __syncthreads();
  if (tid < 8) {
    float v = 0.f;
#pragma unroll
    for (int w = 0; w < 4; ++w) v += *(const float*)(lds + X_FIN + (w * 8 + tid) * 4);
    out[bB * 512 + 8 * ii + tid] = (u16)bfbits(v);
  }
  if (bid == 0 && wave == 4) {
    for (unsigned spins = 0;; ++spins) {
      bool ok = true;
#pragma unroll
      for (int k = 0; k < 4; ++k) { const v4u fv = LDU(WS_FIN + (unsigned)((k * 64 * 64 + lane) * 4) * 16u); ok &= TAGS_OK(fv); }
      if (__all(ok) || spins > SPIN_CAP) break;
      __builtin_amdgcn_s_sleep(4);
    }
    if (lane == 0) __builtin_amdgcn_raw_buffer_store_b32(T, rs, WS_EPOCH, 0, AUX_SC1);
  }
}
}

extern "C" void kernel_launch(void* const* d_in, const int* in_sizes, int n_in, void* d_out, int out_size, void* d_ws, size_t ws_size,
                              hipStream_t stream) {
  fused_kernel<<<NWG, NT, 0, stream>>>((const u16*)d_in[0], (const u16*)d_in[1], (const u16*)d_in[2], (const u16*)d_in[3], (const u16*)d_in[4],
                                       (const u16*)d_in[5], (const u16*)d_in[7], (const u16*)d_in[8], (const u16*)d_in[9], (const u16*)d_in[10],
                                       (u16*)d_out, (unsigned char*)d_ws);
}
```

```cpp
#include <hip/hip_runtime.h>
#include <hip/hip_bf16.h>
#include <stdint.h>

namespace {
typedef unsigned short u16;
typedef unsigned v4u __attribute__((ext_vector_type(4)));
typedef unsigned v2u __attribute__((ext_vector_type(2)));
typedef float f32x2 __attribute__((ext_vector_type(2)));
typedef float f32x4 __attribute__((ext_vector_type(4)));
typedef float f32x16 __attribute__((ext_vector_type(16)));
typedef short bf16x8 __attribute__((ext_vector_type(8)));
typedef short bf16x4 __attribute__((ext_vector_type(4)));
#define LAS __attribute__((address_space(3)))

constexpr int NWG = 256, NT = 512;
constexpr int FF = 1792;
constexpr unsigned WS_EPOCH = 0;
constexpr unsigned WS_QP = 4096;
constexpr unsigned WS_QA = WS_QP + 32 * 8 * 2048;
constexpr unsigned WS_OP = WS_QA + 4 * 32 * 2048;
constexpr unsigned WS_ST = WS_OP + 32 * 8 * 4 * 32 * 256;
constexpr unsigned WS_F = WS_ST + 32 * 4 * 32 * 16;
constexpr unsigned WS_CP = WS_F + 64 * 896 * 16;
constexpr unsigned WS_FIN = WS_CP + 32 * 8 * 2048;
constexpr unsigned WS_BYTES = WS_FIN + 8 * 4 * 8 * 32 * 64;
constexpr int AUX_SC1 = 16;
constexpr unsigned SPIN_CAP = 1u << 17;

constexpr int ROFF = 131072;
constexpr int R_P = ROFF;
constexpr int R_STG = ROFF + 4096;
constexpr int X_W0 = ROFF + 20480;
constexpr int X_QB = X_W0 + 1024;
constexpr int X_F1 = X_QB + 1024;
constexpr int X_OJ = X_F1 + 2048;
constexpr int X_WW = X_OJ + 2048;
constexpr int X_RED = X_WW + 1024;
constexpr int X_MX = X_RED;
constexpr int X_G = X_RED + 4096;
constexpr int X_SYNC = X_G + 32;
static_assert(X_SYNC + 16 <= 163840, "lds");

__device__ __forceinline__ float bf2f(unsigned v) { return __uint_as_float(v << 16); }
__device__ __forceinline__ unsigned bfbits(float f) { return __builtin_bit_cast(unsigned short, __float2bfloat16(f)); }
__device__ __forceinline__ float rbf(float f) { return bf2f(bfbits(f)); }
__device__ __forceinline__ unsigned pk2(float a, float b) { return bfbits(a) | (bfbits(b) << 16); }
__device__ __forceinline__ float lo16(unsigned v) { return __uint_as_float(v << 16); }
__device__ __forceinline__ float hi16(unsigned v) { return __uint_as_float(v & 0xffff0000u); }
__device__ __forceinline__ v4u ldnt(const u16* p) { return __builtin_nontemporal_load((const v4u*)p); }
__device__ __forceinline__ int swz16(int r) { return ((r & 3) << 2) | ((((~((r >> 3) ^ (r >> 2))) & 1) << 1) | ((r >> 3) & 1)); }
__device__ __forceinline__ f32x4 mfma16(bf16x8 a, bf16x8 b, f32x4 c) { return __builtin_amdgcn_mfma_f32_16x16x32_bf16(a, b, c, 0, 0, 0); }
__device__ __forceinline__ f32x16 mfma32(bf16x8 a, bf16x8 b, f32x16 c) { return __builtin_amdgcn_mfma_f32_32x32x16_bf16(a, b, c, 0, 0, 0); }
template <int CTRL> __device__ __forceinline__ float dpp(float v) {
  return __uint_as_float(__builtin_amdgcn_update_dpp(0, __float_as_uint(v), CTRL, 0xf, 0xf, true));
}
__device__ __forceinline__ float x32(float a) {
  auto r = __builtin_amdgcn_permlane32_swap(__float_as_uint(a), __float_as_uint(a), false, false);
  return __uint_as_float((threadIdx.x & 32) ? r[0] : r[1]);
}
__device__ __forceinline__ float x16(float a) {
  auto r = __builtin_amdgcn_permlane16_swap(__float_as_uint(a), __float_as_uint(a), false, false);
  return __uint_as_float((threadIdx.x & 16) ? r[0] : r[1]);
}
__device__ __forceinline__ float row_sum(float v) {
  v += dpp<0xB1>(v); v += dpp<0x4E>(v); v += dpp<0x141>(v); v += dpp<0x140>(v); return v;
}
__device__ __forceinline__ float row_max(float v) {
  v = fmaxf(v, dpp<0xB1>(v)); v = fmaxf(v, dpp<0x4E>(v)); v = fmaxf(v, dpp<0x141>(v)); v = fmaxf(v, dpp<0x140>(v)); return v;
}
template <int S> __device__ __forceinline__ bf16x8 colfrag(const v4u* w) {
  constexpr unsigned sel = (S & 1) ? 0x07060302u : 0x05040100u;
  v4u r;
#pragma unroll
  for (int i = 0; i < 4; ++i) r[i] = __builtin_amdgcn_perm(w[2 * i + 1][S >> 1], w[2 * i][S >> 1], sel);
  return __builtin_bit_cast(bf16x8, r);
}
__device__ __forceinline__ void gemv_block(bf16x8 a, const v4u* w, f32x4 (&acc)[8]) {
  acc[0] = mfma16(a, colfrag<0>(w), acc[0]); acc[1] = mfma16(a, colfrag<1>(w), acc[1]);
  acc[2] = mfma16(a, colfrag<2>(w), acc[2]); acc[3] = mfma16(a, colfrag<3>(w), acc[3]);
  acc[4] = mfma16(a, colfrag<4>(w), acc[4]); acc[5] = mfma16(a, colfrag<5>(w), acc[5]);
  acc[6] = mfma16(a, colfrag<6>(w), acc[6]); acc[7] = mfma16(a, colfrag<7>(w), acc[7]);
}
__device__ __forceinline__ void to_frags(v4u* w) {
  v4u r[8];
  r[0] = __builtin_bit_cast(v4u, colfrag<0>(w)); r[1] = __builtin_bit_cast(v4u, colfrag<1>(w));
  r[2] = __builtin_bit_cast(v4u, colfrag<2>(w)); r[3] = __builtin_bit_cast(v4u, colfrag<3>(w));
  r[4] = __builtin_bit_cast(v4u, colfrag<4>(w)); r[5] = __builtin_bit_cast(v4u, colfrag<5>(w));
  r[6] = __builtin_bit_cast(v4u, colfrag<6>(w)); r[7] = __builtin_bit_cast(v4u, colfrag<7>(w));
#pragma unroll
  for (int i = 0; i < 8; ++i) w[i] = r[i];
}
__device__ __forceinline__ void gemv_frags(bf16x8 a, const v4u* f, f32x4 (&acc)[8]) {
#pragma unroll
  for (int i = 0; i < 8; ++i) acc[i] = mfma16(a, __builtin_bit_cast(bf16x8, f[i]), acc[i]);
}
#define SB0() __builtin_amdgcn_sched_barrier(0)
#define TAGS_OK(u) (((u)[0] == T) & ((u)[2] == T))
#define LDU(off) __builtin_amdgcn_raw_buffer_load_b128(rs, (off), 0, AUX_SC1)
#define STU(v, off) __builtin_amdgcn_raw_buffer_store_b128((v), rs, (off), 0, AUX_SC1)


__device__ __forceinline__ void publish_q(const f32x4 (&acc)[8], unsigned char* priv, __amdgpu_buffer_rsrc_t rs, unsigned off, unsigned T, int lane) {
  if (lane < 16) {
#pragma unroll
    for (int b = 0; b < 4; ++b)
      *(v4u*)(priv + b * 256 + lane * 16) = v4u{pk2(acc[0][b], acc[1][b]), pk2(acc[2][b], acc[3][b]), pk2(acc[4][b], acc[5][b]), pk2(acc[6][b], acc[7][b])};
  }
#pragma unroll
  for (int k = 0; k < 2; ++k) {
    const v2u u = *(const v2u*)(priv + (k * 64 + lane) * 8);
    STU((v4u{T, u[0], T, u[1]}), off + (unsigned)((k * 64 + lane) * 16));
  }
}
__device__ __forceinline__ void gather_q(unsigned char* lds, int cnt, __amdgpu_buffer_rsrc_t rs, unsigned off, unsigned T, int wave, int lane) {
  v4u g[4];
  const unsigned o = off + (unsigned)(((lane >> 5) * 4 * 128 + 32 * wave + (lane & 31)) * 16);
  for (unsigned spins = 0;; ++spins) {
    bool ok = true;
#pragma unroll
    for (int i = 0; i < 4; ++i) g[i] = LDU(o + i * 2048u);
#pragma unroll
    for (int i = 0; i < 4; ++i) ok &= TAGS_OK(g[i]);
    if (__all(ok) || spins > SPIN_CAP) break;
    __builtin_amdgcn_s_sleep(2);
  }
  f32x4 a = {0.f, 0.f, 0.f, 0.f};
#pragma unroll
  for (int i = 0; i < 4; ++i) { a[0] += lo16(g[i][1]); a[1] += hi16(g[i][1]); a[2] += lo16(g[i][3]); a[3] += hi16(g[i][3]); }
#pragma unroll
  for (int c = 0; c < 4; ++c) a[c] += x32(a[c]);
  if (lane < 32) *(v2u*)(lds + X_QB + (32 * wave + lane) * 8) = v2u{pk2(a[0], a[1]), pk2(a[2], a[3])};
  __builtin_amdgcn_fence(__ATOMIC_RELEASE, "workgroup");
  if (lane == 0) __hip_atomic_fetch_add((LAS unsigned*)(lds + X_SYNC + cnt * 4), 1u, __ATOMIC_RELAXED, __HIP_MEMORY_SCOPE_WORKGROUP);
}
__device__ __forceinline__ void wait4(unsigned char* lds, int cnt) {
  while (__hip_atomic_load((LAS unsigned*)(lds + X_SYNC + cnt * 4), __ATOMIC_RELAXED, __HIP_MEMORY_SCOPE_WORKGROUP) < 4u) __builtin_amdgcn_s_sleep(1);
  __builtin_amdgcn_fence(__ATOMIC_ACQUIRE, "workgroup");
}

__global__ void __launch_bounds__(NT, 2)
fused_kernel(const u16* __restrict__ x, const u16* __restrict__ ckv, const u16* __restrict__ wq, const u16* __restrict__ wuk,
             const u16* __restrict__ wuv, const u16* __restrict__ wo, const u16* __restrict__ wg, const u16* __restrict__ wu,
             const u16* __restrict__ wd, const u16* __restrict__ gw, u16* __restrict__ out, unsigned char* ws) {
  __shared__ __attribute__((aligned(16))) unsigned char lds[163840];
  const int tid = threadIdx.x, lane = tid & 63, wave = __builtin_amdgcn_readfirstlane(tid >> 6);
  const int bid = blockIdx.x, xcd = bid & 7, idx = bid >> 3;
  const int hA = xcd * 4 + (idx >> 3), s8 = idx & 7;
  const int pid = xcd * 16 + (idx >> 1), b3 = pid >> 5, j3 = pid & 31, hf3 = idx & 1;
  const int r15 = lane & 15, g4 = lane >> 4, hh = lane >> 5;
  const __amdgpu_buffer_rsrc_t rs = __builtin_amdgcn_make_buffer_rsrc(ws, 0, WS_BYTES, 0x00020000);
  const f32x4 Z4 = {0.f, 0.f, 0.f, 0.f};
  const v4u ZU = {0u, 0u, 0u, 0u};
  const bf16x8 ZB = {0, 0, 0, 0, 0, 0, 0, 0};

  const unsigned epoch = __builtin_amdgcn_raw_buffer_load_b32(rs, WS_EPOCH, 0, AUX_SC1);
  if (tid < 3) *(volatile LAS unsigned*)(lds + X_SYNC + tid * 4) = 0u;
  __syncthreads();
  v4u big[32];
  unsigned T;
  if (wave >= 4) {
    while (__hip_atomic_load((LAS unsigned*)(lds + X_SYNC + 8), __ATOMIC_RELAXED, __HIP_MEMORY_SCOPE_WORKGROUP) == 0u) __builtin_amdgcn_s_sleep(1);
    const u16* p = ckv + ((size_t)b3 * 4096 + j3 * 128 + 32 * (wave - 4)) * 512 + lane * 8;
#pragma unroll
    for (int i = 0; i < 32; ++i) big[i] = ldnt(p + i * 512);
    SB0();
    T = __builtin_amdgcn_readfirstlane(epoch) + 1u;
#pragma unroll
    for (int i = 0; i < 32; ++i)
      *(v4u*)(lds + (32 * (wave - 4) + i) * 1024 + ((lane ^ swz16(i & 15)) << 4)) = big[i];
  } else {
    v4u aux[4];
    {
      const u16* q = wuk + ((size_t)(s8 * 64 + 16 * wave + r15) * 32 + hA) * 128 + g4 * 8;
      if (wave == 0) {
        v4u xa0 = ZU, xa1 = ZU;
        const int m0 = 64 * s8 + 8 * g4;
        if (r15 < 4) { xa0 = *(const v4u*)(x + r15 * 512 + m0); xa1 = *(const v4u*)(x + r15 * 512 + m0 + 32); }
        const u16* p = wq + (size_t)m0 * 4096 + hA * 128 + r15 * 8;
#pragma unroll
        for (int i = 0; i < 8; ++i) { big[i] = ldnt(p + (size_t)i * 4096); big[8 + i] = ldnt(p + (size_t)(32 + i) * 4096); }
#pragma unroll
        for (int k = 0; k < 4; ++k) aux[k] = ldnt(q + k * 32);
        SB0();
        T = __builtin_amdgcn_readfirstlane(epoch) + 1u;
        f32x4 acc[8] = {Z4, Z4, Z4, Z4, Z4, Z4, Z4, Z4};
        gemv_block(__builtin_bit_cast(bf16x8, xa0), big, acc);
        gemv_block(__builtin_bit_cast(bf16x8, xa1), big + 8, acc);
        if (lane == 0) __hip_atomic_store((LAS unsigned*)(lds + X_SYNC + 8), 1u, __ATOMIC_RELAXED, __HIP_MEMORY_SCOPE_WORKGROUP);
        publish_q(acc, lds + X_W0, rs, WS_QP + (unsigned)((hA * 8 + s8) * 2048), T, lane);
      } else {
#pragma unroll
        for (int k = 0; k < 4; ++k) aux[k] = ldnt(q + k * 32);
        T = __builtin_amdgcn_readfirstlane(epoch) + 1u;
        if (wave == 2) {
      if (bid < 224) {
        const int fs = bid >> 3, mr = bid & 7, f0 = 64 * mr + 8 * g4;
        v4u xa0 = ZU, xa1 = ZU;
        if (r15 < 4) { xa0 = *(const v4u*)(x + r15 * 512 + f0); xa1 = *(const v4u*)(x + r15 * 512 + f0 + 32); }
        const u16* q = (r15 < 8 ? wg : wu) + (size_t)f0 * FF + 64 * fs + 8 * (r15 & 7);
#pragma unroll
        for (int i = 0; i < 8; ++i) { big[i] = ldnt(q + (size_t)i * FF); big[8 + i] = ldnt(q + (size_t)(32 + i) * FF); }
        f32x4 acc[8] = {Z4, Z4, Z4, Z4, Z4, Z4, Z4, Z4};
        gemv_block(__builtin_bit_cast(bf16x8, xa0), big, acc);
        gemv_block(__builtin_bit_cast(bf16x8, xa1), big + 8, acc);
        unsigned char* priv = lds + X_F1;
        if (lane < 16) {
#pragma unroll
          for (int b = 0; b < 4; ++b) {
            float* d = (float*)priv + b * 128 + lane * 8;
            *(f32x4*)d = f32x4{acc[0][b], acc[1][b], acc[2][b], acc[3][b]};
            *(f32x4*)(d + 4) = f32x4{acc[4][b], acc[5][b], acc[6][b], acc[7][b]};
          }
        }
#pragma unroll
        for (int b = 0; b < 4; ++b) {
          const f32x2 u = *(const f32x2*)(priv + (b * 64 + lane) * 8);
          STU((v4u{T, __float_as_uint(u[0]), T, __float_as_uint(u[1])}), WS_F + (unsigned)((((mr * 4 + b) * 2 + hh) * 896 + 32 * fs + (lane & 31)) * 16));
        }
      }
        } else if (wave == 3) {
      v4u xr[4];
#pragma unroll
      for (int b = 0; b < 4; ++b) xr[b] = *(const v4u*)(x + b * 512 + lane * 8);
      const v4u gr = *(const v4u*)(gw + lane * 8);
#pragma unroll
      for (int b = 0; b < 4; ++b) {
        float s = 0.f;
#pragma unroll
        for (int j = 0; j < 4; ++j) s += rbf(lo16(xr[b][j]) * lo16(gr[j])) + rbf(hi16(xr[b][j]) * hi16(gr[j]));
        s = row_sum(s); s += x16(s); s += x32(s);
        if (lane == 0) {
          const float g = rbf(1.f / (1.f + __expf(-rbf(s))));
          *(f32x2*)(lds + X_G + b * 8) = f32x2{g, rbf(1.f - g)};
        }
      }
        }
      }
    }
    gather_q(lds, 0, rs, WS_QP + (unsigned)(hA * 8 * 2048), T, wave, lane);
    wait4(lds, 0);
    {
      f32x4 acc = Z4;
#pragma unroll
      for (int k = 0; k < 4; ++k) {
        bf16x8 bq = ZB;
        if (r15 < 4) bq = *(const bf16x8*)(lds + X_QB + (r15 * 128 + 32 * k + 8 * g4) * 2);
        acc = mfma16(__builtin_bit_cast(bf16x8, aux[k]), bq, acc);
      }
      const float sc = 0.08838834764831845f * 1.4426950408889634f;
      if (r15 < 4) STU((v4u{T, pk2(acc[0] * sc, acc[1] * sc), T, pk2(acc[2] * sc, acc[3] * sc)}), WS_QA + (unsigned)((r15 * 32 + hA) * 2048 + (s8 * 64 + 16 * wave + 4 * g4) * 4));
    }
    if (wave == 0) {
      const u16* p = wuv + ((size_t)(s8 * 64 + 8 * g4) * 32 + hA) * 128 + r15 * 8;
#pragma unroll
      for (int i = 0; i < 8; ++i) { big[i] = ldnt(p + (size_t)i * 4096); big[8 + i] = ldnt(p + (size_t)(32 + i) * 4096); }
    } else if (wave == 1) {
      const u16* p = wo + (size_t)(hA * 128 + 32 * (r15 >> 3) + 8 * g4) * 512 + 64 * s8 + 8 * (r15 & 7);
#pragma unroll
      for (int i = 0; i < 8; ++i) { big[i] = ldnt(p + i * 512); big[8 + i] = ldnt(p + (64 + i) * 512); }
    } else if (wave == 2) {
#pragma unroll
      for (int i = 0; i < 8; ++i) {
        int f = 56 * hA + 32 * (r15 >> 3) + 8 * g4 + i;
        f = f < FF ? f : FF - 1;
        big[i] = ldnt(wd + (size_t)f * 512 + 64 * s8 + 8 * (r15 & 7));
      }
    }
  }

  {
    v4u qg[4];
    for (unsigned spins = 0;; ++spins) {
      bool ok = true;
#pragma unroll
      for (int k = 0; k < 4; ++k) qg[k] = LDU(WS_QA + (unsigned)((b3 * 32 + 16 * hf3 + 2 * wave + (k >> 1)) * 2048 + (k & 1) * 1024 + lane * 16));
#pragma unroll
      for (int k = 0; k < 4; ++k) ok &= TAGS_OK(qg[k]);
      if (__all(ok) || spins > SPIN_CAP) break;
      __builtin_amdgcn_s_sleep(4);
    }
#pragma unroll
    for (int k = 0; k < 4; ++k) {
      const int h = 2 * wave + (k >> 1), pi = (k & 1) * 64 + lane, ch = pi >> 1;
      *(v2u*)(lds + ROFF + h * 1024 + ((ch ^ swz16(h)) << 4) + (pi & 1) * 8) = v2u{qg[k][1], qg[k][3]};
    }
  }
  __syncthreads();
  f32x4 sacc = Z4;
  {
    const int sw = swz16(r15);
#pragma unroll
    for (int ks = 0; ks < 16; ++ks) {
      const int o = r15 * 1024 + (((4 * (ks & 3) + g4) ^ sw) << 4) + (ks >> 2) * 256;
      sacc = mfma16(*(const bf16x8*)(lds + wave * 16384 + o), *(const bf16x8*)(lds + ROFF + o), sacc);
    }
  }
  {
    float m0 = fmaxf(fmaxf(sacc[0], sacc[1]), fmaxf(sacc[2], sacc[3]));
    m0 = fmaxf(m0, x16(m0)); m0 = fmaxf(m0, x32(m0));
    if (lane < 16) *(float*)(lds + X_MX + (wave * 16 + lane) * 4) = m0;
  }
  __syncthreads();
  float mxh = -3.0e38f;
  {
#pragma unroll
    for (int w = 0; w < 8; ++w) mxh = fmaxf(mxh, *(const float*)(lds + X_MX + (w * 16 + r15) * 4));
    const float p0 = exp2f(sacc[0] - mxh), p1 = exp2f(sacc[1] - mxh), p2 = exp2f(sacc[2] - mxh), p3 = exp2f(sacc[3] - mxh);
    float ls = (p0 + p1) + (p2 + p3);
    *(v2u*)(lds + R_P + r15 * 256 + (((2 * wave + (g4 >> 1)) ^ swz16(r15)) << 4) + (g4 & 1) * 8) = v2u{pk2(p0, p1), pk2(p2, p3)};
    ls += x16(ls); ls += x32(ls);
    if (lane < 16) *(float*)(lds + X_MX + 512 + (wave * 16 + lane) * 4) = ls;
  }
  __syncthreads();
  {
    if (wave == 0 && lane < 16) {
      float l = 0.f;
#pragma unroll
      for (int w = 0; w < 8; ++w) l += *(const float*)(lds + X_MX + 512 + (w * 16 + lane) * 4);
      STU((v4u{T, __float_as_uint(mxh), T, __float_as_uint(l)}), WS_ST + (unsigned)((((16 * hf3 + lane) * 4 + b3) * 32 + j3) * 16));
    }
    const int q = r15 >> 2, p = lane & 3;
    bf16x8 pb[4];
#pragma unroll
    for (int ks = 0; ks < 4; ++ks) pb[ks] = *(const bf16x8*)(lds + R_P + r15 * 256 + (((4 * ks + g4) ^ swz16(r15)) << 4));
    unsigned char* stg = lds + R_STG + wave * 2048;
#pragma unroll
    for (int cp = 0; cp < 2; ++cp) {
#pragma unroll
      for (int c2 = 0; c2 < 2; ++c2) {
        const int cb = 2 * cp + c2;
        const int chunk = 8 * wave + 2 * cb + (p >> 1);
        f32x4 o = Z4;
#pragma unroll
        for (int ks = 0; ks < 4; ++ks) {
          const int r0 = 8 * g4 + q, r1 = r0 + 4;
          const bf16x4 a0 = __builtin_amdgcn_ds_read_tr16_b64_v4i16((LAS bf16x4*)(lds + (32 * ks + r0) * 1024 + ((chunk ^ swz16(r0 & 15)) << 4) + 8 * (p & 1)));
          const bf16x4 a1 = __builtin_amdgcn_ds_read_tr16_b64_v4i16((LAS bf16x4*)(lds + (32 * ks + r1) * 1024 + ((chunk ^ swz16(r1 & 15)) << 4) + 8 * (p & 1)));
          o = mfma16(bf16x8{a0[0], a0[1], a0[2], a0[3], a1[0], a1[1], a1[2], a1[3]}, pb[ks], o);
        }
        *(v4u*)(stg + r15 * 128 + (((4 * c2 + g4) ^ (r15 & 7)) << 4)) = v4u{T, pk2(o[0], o[1]), T, pk2(o[2], o[3])};
      }
#pragma unroll
      for (int i = 0; i < 2; ++i) {
        const int h2 = 8 * i + (lane >> 3), u = lane & 7;
        const v4u o = *(const v4u*)(stg + h2 * 128 + ((u ^ (h2 & 7)) << 4));
        STU(o, WS_OP + (unsigned)((((((16 * hf3 + h2) * 8 + wave) * 4 + b3) * 32 + j3) * 256) + cp * 128 + u * 16));
      }
    }
  }
  if (wave < 2) { to_frags(big); to_frags(big + 8); } else if (wave == 2) to_frags(big);
  if (wave >= 4) {
    const int t = tid - 256, gq = t >> 5, u = t & 31;
    v4u fg[8];
    for (unsigned spins = 0;; ++spins) {
      bool ok = true;
#pragma unroll
      for (int k = 0; k < 8; ++k) { fg[k] = v4u{T, 0u, T, 0u}; if (u < 28) fg[k] = LDU(WS_F + (unsigned)(((k * 8 + gq) * 896 + 28 * hA + u) * 16)); }
#pragma unroll
      for (int k = 0; k < 8; ++k) ok &= TAGS_OK(fg[k]);
      if (__all(ok) || spins > SPIN_CAP) break;
      __builtin_amdgcn_s_sleep(4);
    }
    float a0 = 0.f, a1 = 0.f;
#pragma unroll
    for (int k = 0; k < 8; ++k) { a0 += __uint_as_float(fg[k][1]); a1 += __uint_as_float(fg[k][3]); }
    *(f32x2*)(lds + X_F1 + (gq * 64 + 2 * u) * 4) = f32x2{a0, a1};
  }
  SB0();
  {
    const int b = wave >> 1, jh = wave & 1;
    v4u og[4], st;
    for (unsigned spins = 0;; ++spins) {
      st = LDU(WS_ST + (unsigned)(((hA * 4 + b) * 32 + (lane & 31)) * 16));
      if (__all(TAGS_OK(st)) || spins > SPIN_CAP) break;
      __builtin_amdgcn_s_sleep(2);
    }
    for (unsigned spins = 0;; ++spins) {
      bool ok = true;
#pragma unroll
      for (int i = 0; i < 4; ++i) og[i] = LDU(WS_OP + (unsigned)((((hA * 8 + s8) * 4 + b) * 32 + 16 * jh + 4 * i + g4) * 256 + r15 * 16));
#pragma unroll
      for (int i = 0; i < 4; ++i) ok &= TAGS_OK(og[i]);
      if (__all(ok) || spins > SPIN_CAP) break;
      __builtin_amdgcn_s_sleep(2);
    }
    const float mj = __uint_as_float(st[1]), lj = __uint_as_float(st[3]);
    float mm = row_max(mj); mm = fmaxf(mm, x16(mm));
    const float e = exp2f(mj - mm);
    float L = row_sum(e * lj); L += x16(L);
    float* ww = (float*)(lds + X_WW) + wave * 32;
    if (lane < 32) ww[lane] = e / L;
    f32x4 ov = Z4;
#pragma unroll
    for (int i = 0; i < 4; ++i) {
      const float w = ww[16 * jh + 4 * i + g4];
      ov[0] += lo16(og[i][1]) * w; ov[1] += hi16(og[i][1]) * w; ov[2] += lo16(og[i][3]) * w; ov[3] += hi16(og[i][3]) * w;
    }
#pragma unroll
    for (int c = 0; c < 4; ++c) { ov[c] += x16(ov[c]); ov[c] += x32(ov[c]); }
    if (lane < 16) *(f32x4*)(lds + X_OJ + (wave * 64 + lane * 4) * 4) = ov;
  }
  __syncthreads();
  float* red = (float*)(lds + X_RED);
  if (wave < 4) {
    if (wave == 0) {
      f32x4 acc[8] = {Z4, Z4, Z4, Z4, Z4, Z4, Z4, Z4};
#pragma unroll
      for (int blk = 0; blk < 2; ++blk) {
        v4u a = ZU;
        if (r15 < 8) {
          const float* o = (const float*)(lds + X_OJ) + (r15 & 3) * 128 + 32 * blk + 8 * g4;
          const f32x4 p0 = *(const f32x4*)o + *(const f32x4*)(o + 64), p1 = *(const f32x4*)(o + 4) + *(const f32x4*)(o + 68);
          a = v4u{pk2(p0[0], p0[1]), pk2(p0[2], p0[3]), pk2(p1[0], p1[1]), pk2(p1[2], p1[3])};
          if (r15 >= 4) a = v4u{pk2(p0[0] - lo16(a[0]), p0[1] - hi16(a[0])), pk2(p0[2] - lo16(a[1]), p0[3] - hi16(a[1])),
                                pk2(p1[0] - lo16(a[2]), p1[1] - hi16(a[2])), pk2(p1[2] - lo16(a[3]), p1[3] - hi16(a[3]))};
        }
        gemv_frags(__builtin_bit_cast(bf16x8, a), big + 8 * blk, acc);
      }
#pragma unroll
      for (int s = 0; s < 8; ++s)
#pragma unroll
        for (int b = 0; b < 4; ++b) acc[s][b] += x16(acc[s][b]);
      publish_q(acc, lds + X_W0, rs, WS_CP + (unsigned)((hA * 8 + s8) * 2048), T, lane);
    } else if (wave == 2) {
      v4u a = ZU;
      if (r15 < 8) {
        const float* fgs = (const float*)(lds + X_F1) + (r15 & 3) * 128 + 32 * (r15 >> 2) + 8 * g4;
        float hv[8];
#pragma unroll
        for (int j = 0; j < 8; ++j) {
          float ga = rbf(fgs[j]), ua = rbf(fgs[64 + j]);
          ga = ga > 0.f ? ga : 0.f;
          hv[j] = ga * ua;
        }
        a = v4u{pk2(hv[0], hv[1]), pk2(hv[2], hv[3]), pk2(hv[4], hv[5]), pk2(hv[6], hv[7])};
      }
      f32x4 acc[8] = {Z4, Z4, Z4, Z4, Z4, Z4, Z4, Z4};
      gemv_frags(__builtin_bit_cast(bf16x8, a), big, acc);
      if (lane < 8 || (lane >= 24 && lane < 32)) {
        float* d = red + 512 + (lane >> 4) * 256 + (lane & 7) * 8;
#pragma unroll
        for (int b = 0; b < 4; ++b) {
          *(f32x4*)(d + b * 64) = f32x4{acc[0][b], acc[1][b], acc[2][b], acc[3][b]};
          *(f32x4*)(d + b * 64 + 4) = f32x4{acc[4][b], acc[5][b], acc[6][b], acc[7][b]};
        }
      }
    }
    gather_q(lds, 1, rs, WS_CP + (unsigned)(hA * 8 * 2048), T, wave, lane);
    if (wave == 1) {
      wait4(lds, 1);
      bf16x8 a0 = ZB, a1 = ZB;
      if (r15 < 8) {
        a0 = *(const bf16x8*)(lds + X_QB + ((r15 & 3) * 128 + 32 * (r15 >> 2) + 8 * g4) * 2);
        a1 = *(const bf16x8*)(lds + X_QB + ((r15 & 3) * 128 + 64 + 32 * (r15 >> 2) + 8 * g4) * 2);
      }
      f32x4 acc[8] = {Z4, Z4, Z4, Z4, Z4, Z4, Z4, Z4};
      gemv_frags(a0, big, acc);
      gemv_frags(a1, big + 8, acc);
      if (lane < 8 || (lane >= 24 && lane < 32)) {
        float* d = red + (lane >> 4) * 256 + (lane & 7) * 8;
#pragma unroll
        for (int b = 0; b < 4; ++b) {
          *(f32x4*)(d + b * 64) = f32x4{acc[0][b], acc[1][b], acc[2][b], acc[3][b]};
          *(f32x4*)(d + b * 64 + 4) = f32x4{acc[4][b], acc[5][b], acc[6][b], acc[7][b]};
        }
      }
      {
        const f32x2 gate = *(const f32x2*)(lds + X_G + g4 * 8);
        const float* r = red + g4 * 64 + r15 * 4;
        const f32x4 sa = *(const f32x4*)r + *(const f32x4*)(r + 256), sb = *(const f32x4*)(r + 512) + *(const f32x4*)(r + 768);
        const f32x4 v = sa * gate[0] + sb * gate[1];
        const unsigned o = WS_FIN + (unsigned)(((((s8 * 4 + g4) * 8 + (r15 >> 1)) * 32 + hA) * 4 + (r15 & 1) * 2) * 16);
        STU((v4u{T, __float_as_uint(v[0]), T, __float_as_uint(v[1])}), o);
        STU((v4u{T, __float_as_uint(v[2]), T, __float_as_uint(v[3])}), o + 16);
      }
      v4u f0, f1;
      for (unsigned spins = 0;; ++spins) {
        f0 = LDU(WS_FIN + (unsigned)(((s8 * 32 + hA) * 128 + lane) * 16));
        f1 = LDU(WS_FIN + (unsigned)(((s8 * 32 + hA) * 128 + 64 + lane) * 16));
        if (__all(TAGS_OK(f0) & TAGS_OK(f1)) || spins > SPIN_CAP) break;
        __builtin_amdgcn_s_sleep(1);
      }
      float w0 = __uint_as_float(f0[1]) + __uint_as_float(f1[1]), w1 = __uint_as_float(f0[3]) + __uint_as_float(f1[3]);
      w0 += dpp<0x124>(w0); w1 += dpp<0x124>(w1);
      w0 += dpp<0x128>(w0); w1 += dpp<0x128>(w1);
      w0 += x16(w0); w1 += x16(w1);
      w0 += x32(w0); w1 += x32(w1);
      if (lane < 4) *(unsigned*)(out + (hA >> 3) * 512 + 64 * s8 + 8 * (hA & 7) + 2 * lane) = pk2(w0, w1);
    }
  } else if (wave == 4 && bid == 0) {
    for (unsigned spins = 0;; ++spins) {
      bool ok = true;
#pragma unroll
      for (int k = 0; k < 4; ++k) { const int t = k * 64 + lane; const v4u fv = LDU(WS_FIN + (unsigned)((((t >> 5) * 32 * 32 + (t & 31)) * 4) * 16)); ok &= TAGS_OK(fv); }
      if (__all(ok) || spins > SPIN_CAP) break;
      __builtin_amdgcn_s_sleep(4);
    }
    if (lane == 0) __builtin_amdgcn_raw_buffer_store_b32(T, rs, WS_EPOCH, 0, AUX_SC1);
  }

}
}

extern "C" void kernel_launch(void* const* d_in, const int* in_sizes, int n_in, void* d_out, int out_size, void* d_ws, size_t ws_size,
                              hipStream_t stream) {
  fused_kernel<<<NWG, NT, 0, stream>>>((const u16*)d_in[0], (const u16*)d_in[1], (const u16*)d_in[2], (const u16*)d_in[3], (const u16*)d_in[4],
                                       (const u16*)d_in[5], (const u16*)d_in[7], (const u16*)d_in[8], (const u16*)d_in[9], (const u16*)d_in[10],
                                       (u16*)d_out, (unsigned char*)d_ws);
}
```
